```python
import torch

def _softmax(x, axis=-1):
    return torch.softmax(x, dim=axis)
def _gelu(x):
    return torch.nn.functional.gelu(x, approximate="tanh")
def _silu(x):
    return torch.nn.functional.silu(x)
def _sigmoid(x):
    return torch.sigmoid(x)
def _layernorm(x, axis=-1, eps=1e-5):
    m = x.mean(dim=axis, keepdim=True)
    v = x.var(dim=axis, keepdim=True, unbiased=False)
    return (x - m) / torch.sqrt(v + eps)
def _scatter_add(tgt, idx, src, axis=0):
    return tgt.clone().index_add_(axis, idx, src)
def _segment_sum(src, seg, n):
    out = torch.zeros((n,) + src.shape[1:], dtype=src.dtype)
    return out.index_add_(0, seg, src)
def _pad(x, pw):
    flat = tuple(v for pair in reversed(pw) for v in pair)
    return torch.nn.functional.pad(x, flat)

kernel_name = 'sequential_preLN__sliding_local_dilated__mixer_channel_mlp__prompt'

def setup_inputs():

    _w = (lambda a: (lambda t: t.to(torch.bfloat16) if t.is_floating_point() else t)(torch.as_tensor(a)))
    def _A_setup():
        B, S, D = 1, 2048, 512
        return {
            "x": _w((lambda *s: torch.randn(*s, dtype=torch.bfloat16) * 0.05)(B, S, D)),
            "wqkv": _w((lambda *s: torch.randn(*s, dtype=torch.bfloat16) * 0.05)(D, 3 * D)),
            "wo": _w((lambda *s: torch.randn(*s, dtype=torch.bfloat16) * 0.05)(D, D)),
        }
    _A_in = _A_setup()

    def _B_setup():
        B, S, D, F = 1, 2048, 512, 1792
        return {
            "x": _w((lambda *s: torch.randn(*s, dtype=torch.bfloat16) * 0.05)(B, S, D)),
            "w1": _w((lambda *s: torch.randn(*s, dtype=torch.bfloat16) * 3.2)(D, F) * (D ** -0.5)),
            "b1": _w((lambda *s: torch.randn(*s, dtype=torch.bfloat16) * 1.6)(F)),
            "w2": _w((lambda *s: torch.randn(*s, dtype=torch.bfloat16) * 0.2)(F, D) * (F ** -0.5)),
            "b2": _w((lambda *s: torch.randn(*s, dtype=torch.bfloat16) * 0.2)(D)),
        }
    _B_in = _B_setup()

    return {
        "A_x": _A_in["x"],
        "A_wqkv": _A_in["wqkv"],
        "A_wo": _A_in["wo"],
        "B_x": _B_in["x"],
        "B_w1": _B_in["w1"],
        "B_b1": _B_in["b1"],
        "B_w2": _B_in["w2"],
        "B_b2": _B_in["b2"],
    }

def reference(A_x, A_wqkv, A_wo, B_x, B_w1, B_b1, B_w2, B_b2):
    def _A_ref(x, wqkv, wo):
        B, S, D = x.shape
        H = 32; Dh = D // H
        W, dil = 8, 2
        qkv = (lambda x, *d: x.permute(*d))((x @ wqkv).reshape(B, S, 3, H, Dh), 2, 0, 3, 1, 4)
        q, k, v = qkv[0], qkv[1], qkv[2]
        scale = Dh ** -0.5
        pos = torch.arange(S)
        off = torch.arange(W) * dil
        idx = pos[:, None] - off[None, :]
        mask = idx >= 0
        idx_c = torch.where(mask, idx, 0).reshape(-1)
        kg = k[:, :, idx_c, :].reshape(B, H, S, W, Dh)
        vg = v[:, :, idx_c, :].reshape(B, H, S, W, Dh)
        logits = torch.einsum("bhsd,bhswd->bhsw", q, kg) * scale
        logits = torch.where(mask[None, None, :, :], logits, -1e9)
        attn = _softmax(logits, axis=-1)
        o = torch.einsum("bhsw,bhswd->bhsd", attn, vg)
        return (lambda x, *d: x.permute(*d))(o, 0, 2, 1, 3).reshape(B, S, D) @ wo

    def _B_ref(x, w1, b1, w2, b2):
        y = _layernorm(x, axis=-1)
        h = _gelu(torch.einsum("bsd,df->bsf", y, w1) + b1)
        return torch.einsum("bsf,fd->bsd", h, w2) + b2

    x = A_x
    h = x + _A_ref(_layernorm(x, axis=-1), A_wqkv, A_wo)
    h2 = h + _B_ref(_layernorm(h, axis=-1), B_w1, B_b1, B_w2, B_b2)
    return h2
```

```cpp
#include <hip/hip_runtime.h>
#include <stdint.h>

#define NO_SKEW
typedef unsigned short u16;
typedef short bf16x8 __attribute__((ext_vector_type(8)));
typedef short bf16x4 __attribute__((ext_vector_type(4)));
typedef __bf16 bfx2 __attribute__((ext_vector_type(2)));
typedef float f32x4 __attribute__((ext_vector_type(4)));
typedef float f32x2 __attribute__((ext_vector_type(2)));
typedef unsigned u32x4 __attribute__((ext_vector_type(4)));
typedef unsigned u32x2 __attribute__((ext_vector_type(2)));

#define DEV __device__ __forceinline__

template <int V> struct IC { static constexpr int value = V; constexpr operator int() const { return V; } };
template <int B, int E, class F> DEV void static_for(F&& f) {
  if constexpr (B < E) { f(IC<B>{}); static_for<B + 1, E>(f); }
}

DEV void glds16(const void* g, void* l) {
  __builtin_amdgcn_global_load_lds((const unsigned*)g, (unsigned*)l, 16, 0, 0);
}
template <int N> DEV void wait_vm() { asm volatile("s_waitcnt vmcnt(%0)" ::"n"(N) : "memory"); }
DEV void bar() { __builtin_amdgcn_s_barrier(); }
DEV void lbar() { asm volatile("s_waitcnt lgkmcnt(0)\n\ts_barrier" ::: "memory"); }

template <int OFF> DEV bf16x8 rd128(unsigned a) {
  bf16x8 r; asm volatile("ds_read_b128 %0, %1 offset:%2" : "=v"(r) : "v"(a), "n"(OFF)); return r;
}
template <int OFF> DEV bf16x4 rdtr(unsigned a) {
  bf16x4 r; asm volatile("ds_read_b64_tr_b16 %0, %1 offset:%2" : "=v"(r) : "v"(a), "n"(OFF)); return r;
}
DEV f32x4 mfma(bf16x8 a, bf16x8 b, f32x4 c) { return __builtin_amdgcn_mfma_f32_16x16x32_bf16(a, b, c, 0, 0, 0); }
DEV bf16x8 cat(bf16x4 a, bf16x4 b) { return __builtin_shufflevector(a, b, 0, 1, 2, 3, 4, 5, 6, 7); }

DEV unsigned pk2(float a, float b) {
  bfx2 v; v[0] = (__bf16)a; v[1] = (__bf16)b; return __builtin_bit_cast(unsigned, v);
}
DEV float bflo(unsigned u) { return __builtin_bit_cast(float, u << 16); }
DEV float bfhi(unsigned u) { return __builtin_bit_cast(float, u & 0xffff0000u); }
DEV float dot2(unsigned a, unsigned b, float c) {
  return __builtin_amdgcn_fdot2_f32_bf16(__builtin_bit_cast(bfx2, a), __builtin_bit_cast(bfx2, b), c, false);
}
DEV void stats8(bf16x8 f, float& s, float& q) {
  u32x4 u = __builtin_bit_cast(u32x4, f);
#pragma unroll
  for (int i = 0; i < 4; ++i) { s = dot2(u[i], 0x3f803f80u, s); q = dot2(u[i], u[i], q); }
}
DEV float red_g(float v) {
  v += __shfl_xor(v, 16); v += __shfl_xor(v, 32); return v;
}

DEV float red_gv(float v) {
  auto a = __builtin_amdgcn_permlane16_swap(__float_as_uint(v), __float_as_uint(v), false, false);
  v = __uint_as_float(a[0]) + __uint_as_float(a[1]);
  auto c = __builtin_amdgcn_permlane32_swap(__float_as_uint(v), __float_as_uint(v), false, false);
  return __uint_as_float(c[0]) + __uint_as_float(c[1]);
}

#define TC(k, i) do {} while (0)
#define TS(k, i) do {} while (0)
template <int P, int MAXN> DEV void wait_chunks(int n) {
  if constexpr (MAXN > 0) {
    if (n >= MAXN) { wait_vm<P * MAXN>(); return; }
    wait_chunks<P, MAXN - 1>(n);
  } else wait_vm<0>();
}
#define ST_MOD ""
#define STR2(x) #x
#define STR(x) STR2(x)
DEV void st128(void* p, u32x4 v) { asm volatile("global_store_dwordx4 %0, %1, off " ST_MOD "\n\ts_nop 1" ::"v"(p), "v"(v) : "memory"); }
DEV void st64(void* p, u32x2 v) { asm volatile("global_store_dwordx2 %0, %1, off " ST_MOD "\n\ts_nop 1" ::"v"(p), "v"(v) : "memory"); }
DEV void wait_lgkm0() { asm volatile("s_waitcnt lgkmcnt(0)" ::: "memory"); }

DEV size_t fragoff(int row, int col, int KS) {
  return ((size_t)((row >> 4) * KS + (col >> 5)) * 64 + ((col >> 3) & 3) * 16 + (row & 15)) * 16 + (col & 7) * 2;
}

DEV void pf_line(unsigned& d, const void* p) { asm volatile("global_load_dword %0, %1, off" : "+v"(d) : "v"(p)); }
DEV void pf_keep(unsigned d) { asm volatile("" ::"v"(d)); }
constexpr int P1_S = 4, P1_A = 80 * 128, P1_ST = P1_A + 3 * 8192;
constexpr int P1_LDS = P1_S * P1_ST, P1_THREADS = 576;
DEV float dpp_x1(float v) {
  return __uint_as_float(__builtin_amdgcn_update_dpp(0u, __float_as_uint(v), 0xB1, 0xf, 0xf, true));
}
template <int C> DEV float dpp_m(float v) {
  return __uint_as_float(__builtin_amdgcn_update_dpp(0u, __float_as_uint(v), C, 0xf, 0xf, true));
}
__global__ __launch_bounds__(576, 1) void k_qkv_attn(const u16* __restrict__ x, const u16* __restrict__ w, u16* __restrict__ o,
                                                    const u16* __restrict__ wo, const u16* __restrict__ w1, const u16* __restrict__ w2,
                                                    unsigned* __restrict__ flags) {
  extern __shared__ __attribute__((aligned(16))) char smem[];
  TS(0, 0);
  const int tid = threadIdx.x, lane = tid & 63, wid = __builtin_amdgcn_readfirstlane(tid >> 6);
  const int r = lane & 15, g = lane >> 4, q = (lane & 15) >> 2, p = lane & 3;
  const int b = blockIdx.x, xcd = b & 7, j = b >> 3;
  const int panel = xcd * 4 + (j >> 3), hq = j & 7;
  const int s0 = panel * 64;
  const int rg = wid & 1, cg = wid >> 1;
  const unsigned sbase = (unsigned)(size_t)smem;
#define PF_WO 0
#define PF_W1 3
  if (wid == 8) {
    unsigned pfd = 0;
    if (b == 0 && lane < 32) flags[lane * 64] = 0u;
    const char* q0 = (const char*)wo + (size_t)b * 2048 + lane * 64;
    const char* q1 = (const char*)w1 + (size_t)b * 7168 + lane * 64;
    bar();
#pragma unroll 1
    for (int c = 0; c < 7; ++c) {
      if (c == PF_WO && lane < 32) pf_line(pfd, q0);
      if (c == PF_W1) { pf_line(pfd, q1); if (lane < 48) pf_line(pfd, q1 + 4096); }
      bar();
    }
    wait_vm<0>(); pf_keep(pfd);
    return;
  }

  const int l3 = lane >> 3, l7 = lane & 7;
  const u16 *pa, *pa2, *pb;
  {
    int gr = s0 - 16 + wid * 8 + l3; gr = gr < 0 ? 0 : gr;
    pa = x + (size_t)gr * 512 + ((l7 ^ l3) << 3);
    pa2 = x + (size_t)(s0 + 48 + (wid & 1) * 8 + l3) * 512 + ((l7 ^ l3) << 3);
    const int fb = ((l3 >> 1) & 1) | ((wid & 1) << 1);
    pb = w + (size_t)(wid * 8 + l3) * 1536 + hq * 64 + (((l7 >> 1) ^ fb) << 4) + (l7 & 1) * 8;
  }
  char* wr = smem + wid * 1024;
#define P1_ROT (xcd + 2 * (j >> 3))
  int ca = (P1_ROT) & 7, cb = ca;
  auto issueA = [&](unsigned so) {
    glds16(pa + ca * 64, wr + so);
    if (wid < 2) glds16(pa2 + ca * 64, wr + so + 8192);
    ca = (ca + 1) & 7;
  };
  auto issueB = [&](unsigned so) {
    const u16* pc = pb + (size_t)cb * (64 * 1536);
    glds16(pc, wr + so + P1_A); glds16(pc + 512, wr + so + P1_A + 8192); glds16(pc + 1024, wr + so + P1_A + 16384);
    cb = (cb + 1) & 7;
  };
#pragma unroll 1
  for (int c = 0; c < P1_S - 1; ++c) { issueA(c * P1_ST); issueB(c * P1_ST); }

  const unsigned aA0 = sbase + (16 + rg * 32 + r) * 128 + (((4 * rg + g) ^ (r & 7)) << 4), aA1 = aA0 ^ 64;
  const unsigned aH = sbase + r * 128 + (((4 * rg + g) ^ (r & 7)) << 4);
  unsigned aB[3];
  {
    const int f = (q >> 1) | ((g & 1) << 1);
#pragma unroll
    for (int i = 0; i < 3; ++i) {
      const int nb = 3 * cg + i;
      aB[i] = sbase + P1_A + (nb >> 2) * 8192 + (8 * g + q) * 128 + (((nb & 3) ^ f) << 5) + p * 8;
    }
  }
  const unsigned h0 = rg * 4096, h1 = 4096 - h0;
  const unsigned aS = sbase + tid * 16;

  f32x4 acc[2][3], acch[3];
#pragma unroll
  for (int i = 0; i < 3; ++i) acc[0][i] = acc[1][i] = acch[i] = f32x4{0, 0, 0, 0};
  float sm = 0.f, sq = 0.f, sm2 = 0.f, sq2 = 0.f;
  bf16x8 xa0[2], xa1[2], xh, xs, xs2;
  bf16x4 wb0[3][2], wb1[3][2];
  xs2 = bf16x8{0, 0, 0, 0, 0, 0, 0, 0};
  auto rd0 = [&](unsigned so) {
    const unsigned a = aA0 + so, sb = so + h0;
    xa0[0] = rd128<0>(a); xa0[1] = rd128<2048>(a);
    xh = rd128<0>(aH + so);
#pragma unroll
    for (int i = 0; i < 3; ++i) { const unsigned bb = aB[i] + sb; wb0[i][0] = rdtr<0>(bb); wb0[i][1] = rdtr<512>(bb); }
  };
  auto rd1 = [&](unsigned so) {
    const unsigned a = aA1 + so, sb = so + h1;
    if (wid < 2) xs2 = rd128<8192>(aS + so);
    xa1[0] = rd128<0>(a); xa1[1] = rd128<2048>(a);
#pragma unroll
    for (int i = 0; i < 3; ++i) { const unsigned bb = aB[i] + sb; wb1[i][0] = rdtr<0>(bb); wb1[i][1] = rdtr<512>(bb); }
    xs = rd128<0>(aS + so);
  };
  auto tie0 = [&]() {
    asm volatile("" : "+v"(xa0[0]), "+v"(xa0[1]), "+v"(xh), "+v"(wb0[0][0]), "+v"(wb0[0][1]), "+v"(wb0[1][0]), "+v"(wb0[1][1]),
                 "+v"(wb0[2][0]), "+v"(wb0[2][1]), "+v"(xs2));
  };
  auto tie1 = [&]() {
    asm volatile("" : "+v"(xa1[0]), "+v"(xa1[1]), "+v"(xs), "+v"(wb1[0][0]), "+v"(wb1[0][1]), "+v"(wb1[1][0]), "+v"(wb1[1][1]),
                 "+v"(wb1[2][0]), "+v"(wb1[2][1]));
  };
  const bf16x8 ones = {0x3f80, 0x3f80, 0x3f80, 0x3f80, 0x3f80, 0x3f80, 0x3f80, 0x3f80};
  auto mma0 = [&]() {
    const bf16x8 xhh = (r == 0) ? ones : xh;
#pragma unroll
    for (int i = 0; i < 3; ++i) {
      const bf16x8 wf = cat(wb0[i][0], wb0[i][1]);
      acc[0][i] = mfma(wf, xa0[0], acc[0][i]); acc[1][i] = mfma(wf, xa0[1], acc[1][i]); acch[i] = mfma(wf, xhh, acch[i]);
    }
    stats8(xs2, sm2, sq2);
  };
  auto mma1 = [&]() {
#pragma unroll
    for (int i = 0; i < 3; ++i) {
      const bf16x8 wf = cat(wb1[i][0], wb1[i][1]);
      acc[0][i] = mfma(wf, xa1[0], acc[0][i]); acc[1][i] = mfma(wf, xa1[1], acc[1][i]);
    }
    stats8(xs, sm, sq);
  };
  static_assert(P1_S == 4, "wait counts");
  auto waitc = [&](int c) {
    if (wid < 2) { if (c < 6) wait_vm<5>(); else wait_vm<0>(); }
    else { if (c < 6) wait_vm<4>(); else wait_vm<0>(); }
  };
  if (wid < 2) wait_vm<10>(); else wait_vm<8>();
  bar();
  TS(0, 1);
  rd0(0);
  unsigned so_rd = 0, so_wr = (P1_S - 1) * P1_ST;
  bool pend = false;
#pragma unroll 1
  for (int c = 0; c < 7; ++c) {
    rd1(so_rd);
    asm volatile("s_waitcnt lgkmcnt(9)" ::: "memory"); tie0();
    mma0();
    if (pend) { issueB(so_wr); so_wr += P1_ST; if (so_wr == P1_S * P1_ST) so_wr = 0; }
    waitc(c); bar();
    so_rd += P1_ST; if (so_rd == P1_S * P1_ST) so_rd = 0;
    rd0(so_rd);
    asm volatile("s_waitcnt lgkmcnt(9)" ::: "memory"); tie1();
    mma1();
    pend = c + P1_S - 1 < 8;
    if (pend) issueA(so_wr);
  }
  rd1(so_rd);
  asm volatile("s_waitcnt lgkmcnt(9)" ::: "memory"); tie0();
  mma0();
  wait_lgkm0(); tie1();
  mma1();
  TS(0, 2);

  constexpr int E_H = 0, E_S = 24576, E_Q = 25600, E_K = E_Q + 64 * 144, E_V = E_K + 80 * 144;
#pragma unroll
  for (int i = 0; i < 3; ++i) *(f32x4*)(smem + E_H + ((wid * 3 + i) * 64 + lane) * 16) = acch[i];
  {
    sm += dpp_m<0xB1>(sm); sq += dpp_m<0xB1>(sq); sm2 += dpp_m<0xB1>(sm2); sq2 += dpp_m<0xB1>(sq2);
    sm += dpp_m<0x4E>(sm); sq += dpp_m<0x4E>(sq); sm2 += dpp_m<0x4E>(sm2); sq2 += dpp_m<0x4E>(sq2);
    sm += dpp_m<0x141>(sm); sq += dpp_m<0x141>(sq); sm2 += dpp_m<0x141>(sm2); sq2 += dpp_m<0x141>(sq2);
    if ((tid & 7) == 0) {
      const float a = sm * (1.f / 512.f);
      *(f32x2*)(smem + E_S + (tid >> 3) * 8) = f32x2{a, rsqrtf(fmaxf(sq * (1.f / 512.f) - a * a, 0.f) + 1e-5f)};
      if (wid < 2) {
        const float a2 = sm2 * (1.f / 512.f);
        *(f32x2*)(smem + E_S + (64 + (tid >> 3)) * 8) = f32x2{a2, rsqrtf(fmaxf(sq2 * (1.f / 512.f) - a2 * a2, 0.f) + 1e-5f)};
      }
    }
  }
  __syncthreads();
  TS(0, 3);
  {
    f32x2 mr[2];
#pragma unroll
    for (int i = 0; i < 2; ++i) mr[i] = *(const f32x2*)(smem + E_S + (16 + rg * 32 + i * 16 + r) * 8);
    const f32x2 mh = *(const f32x2*)(smem + E_S + r * 8);
#pragma unroll
    for (int i = 0; i < 3; ++i) {
      const int nb = 3 * cg + i, seg = nb >> 2, co = (nb & 3) * 32 + g * 8;
      const char* hp0 = smem + E_H + ((cg * 6 + i) * 64) * 16;
      const f32x4 cs = *(const f32x4*)(hp0 + g * 256) + *(const f32x4*)(hp0 + 3072 + g * 256);
      char* img = smem + (seg == 0 ? E_Q - 16 * 144 : seg == 1 ? E_K : E_V) + co;
#pragma unroll
      for (int ii = 0; ii < 2; ++ii) {
        const f32x4 v = (acc[ii][i] - mr[ii][0] * cs) * mr[ii][1];
        *(u32x2*)(img + (16 + rg * 32 + ii * 16 + r) * 144) = u32x2{pk2(v[0], v[1]), pk2(v[2], v[3])};
      }
      if (seg != 0 && (i & 1) == rg) {
        const f32x4 hv = *(const f32x4*)(hp0 + lane * 16) + *(const f32x4*)(hp0 + 3072 + lane * 16);
        const f32x4 v = (hv - mh[0] * cs) * mh[1];
        *(u32x2*)(img + r * 144) = u32x2{pk2(v[0], v[1]), pk2(v[2], v[3])};
      }
    }
  }
  lbar();
  TS(0, 4);
  {
    const int row = tid >> 3, hd = (tid >> 1) & 3, hf = tid & 1, s = s0 + row;
    const char* qp = smem + E_Q + row * 144 + hd * 32;
    const u32x4 q0 = *(const u32x4*)qp, q1 = *(const u32x4*)(qp + 16);
    const char* kp = smem + E_K + (row + 16 - 8 * hf) * 144 + hd * 32;
    float lg[4], lo[4];
#pragma unroll
    for (int i = 0; i < 4; ++i) {
      const u32x4 k0 = *(const u32x4*)(kp - i * 288), k1 = *(const u32x4*)(kp - i * 288 + 16);
      float d = 0.f, d2 = 0.f;
#pragma unroll
      for (int e = 0; e < 4; ++e) { d = dot2(q0[e], k0[e], d); d2 = dot2(q1[e], k1[e], d2); }
      lg[i] = (s - 8 * hf - 2 * i >= 0) ? (d + d2) * 0.25f : -1e9f;
    }
#pragma unroll
    for (int i = 0; i < 4; ++i) lo[i] = dpp_x1(lg[i]);
    float mx = fmaxf(fmaxf(fmaxf(lg[0], lg[1]), fmaxf(lg[2], lg[3])), fmaxf(fmaxf(lo[0], lo[1]), fmaxf(lo[2], lo[3])));
    float sum = 0.f;
#pragma unroll
    for (int i = 0; i < 4; ++i) { lg[i] = __expf(lg[i] - mx); lo[i] = __expf(lo[i] - mx); sum += lg[i] + lo[i]; }
    const float inv = 1.f / sum;
    const char* vp0 = smem + E_V + (row + 16 - 8 * hf) * 144 + hd * 32 + hf * 16;
    const char* vp1 = smem + E_V + (row + 8 + 8 * hf) * 144 + hd * 32 + hf * 16;
    float ov[8] = {0, 0, 0, 0, 0, 0, 0, 0};
#pragma unroll
    for (int i = 0; i < 4; ++i) {
      const u32x4 va = *(const u32x4*)(vp0 - i * 288), vb = *(const u32x4*)(vp1 - i * 288);
#pragma unroll
      for (int e = 0; e < 4; ++e) {
        ov[2 * e] += lg[i] * bflo(va[e]) + lo[i] * bflo(vb[e]);
        ov[2 * e + 1] += lg[i] * bfhi(va[e]) + lo[i] * bfhi(vb[e]);
      }
    }
    st128((char*)o + fragoff(s, hq * 64 + hd * 16 + hf * 8, 16),
          u32x4{pk2(ov[0] * inv, ov[1] * inv), pk2(ov[2] * inv, ov[3] * inv), pk2(ov[4] * inv, ov[5] * inv), pk2(ov[6] * inv, ov[7] * inv)});
  }
  TS(0, 5);
}
template <int OFF> DEV bf16x8 gld128(unsigned voff, const void* sb) {
  bf16x8 r; asm volatile("global_load_dwordx4 %0, %1, %2 offset:%3" : "=v"(r) : "v"(voff), "s"(sb), "n"(OFF)); return r;
}
#define LDSC " sc1"
template <int OFF> DEV bf16x8 gld128c(unsigned voff, const void* sb) {
  bf16x8 r; asm volatile("global_load_dwordx4 %0, %1, %2 offset:%3" LDSC : "=v"(r) : "v"(voff), "s"(sb), "n"(OFF)); return r;
}
template <int OFF> DEV bf16x8 gld128ca(unsigned voff, const void* sb) {
  bf16x8 r; asm volatile("global_load_dwordx4 %0, %1, %2 offset:%3" LDSC : "=a"(r) : "v"(voff), "s"(sb), "n"(OFF)); return r;
}
DEV u32x4 gldu4(const void* p) { u32x4 r; asm volatile("global_load_dwordx4 %0, %1, off" : "=v"(r) : "v"(p)); return r; }
DEV u32x2 gldu2(const void* p) { u32x2 r; asm volatile("global_load_dwordx2 %0, %1, off" : "=v"(r) : "v"(p)); return r; }
DEV f32x2 gldf2c(const void* p) { f32x2 r; asm volatile("global_load_dwordx2 %0, %1, off" LDSC : "=v"(r) : "v"(p)); return r; }
DEV void st128c(void* p, u32x4 v) { asm volatile("global_store_dwordx4 %0, %1, off sc1\n\ts_nop 1" ::"v"(p), "v"(v) : "memory"); }
DEV void st64c(void* p, u32x2 v) { asm volatile("global_store_dwordx2 %0, %1, off sc1\n\ts_nop 1" ::"v"(p), "v"(v) : "memory"); }
DEV void poll_ge(const unsigned* f, unsigned target) {
#pragma unroll 1
  for (int it = 0; it < (1 << 16); ++it) {
    unsigned v;
    asm volatile("global_load_dword %0, %1, off sc1\n\ts_waitcnt vmcnt(0)" : "=v"(v) : "v"(f) : "memory");
    if ((unsigned)__builtin_amdgcn_readfirstlane(v) >= target) break;
    __builtin_amdgcn_s_sleep(1);
  }
}
#define X_PFAT 0
#define SB() __builtin_amdgcn_sched_barrier(0)
constexpr int SKW = 0;
#define X_PRE 4
#define X_DA 8
constexpr int F_LDS = 163840, F_DW = 10, F_DA = X_DA, F_PRE = X_PRE;
constexpr int p4_n(int s) { int n = 4 * F_DA; for (int t = 0; t < s; ++t) n += (t + F_DA < 14 ? 4 : 0) + (t + F_DW < 14 ? 4 : 0); return n; }
constexpr int p4_a(int u) { return u < F_DA ? 4 * (u + 1) : p4_n(u - F_DA) + 4; }
constexpr int p4_w(int v) { return v < F_DW ? 0 : p4_n(v - F_DW) + (v - F_DW + F_DA < 14 ? 4 : 0) + 4; }
constexpr int imax(int a, int b) { return a > b ? a : b; }
#define X_S0 4
constexpr int M_S0 = X_S0, M_C = 8 - M_S0, M_LA = 4;
static_assert(M_C >= 4, "event stages");
constexpr int m_g(int n) { return 7 * M_S0 + n * M_C; }
constexpr int m_s(int b) { return b < 7 * M_S0 ? b / 7 : M_S0 + (b - 7 * M_S0) % M_C; }
constexpr int m_n(int b) { return b < 7 * M_S0 ? b % 7 : (b - 7 * M_S0) / M_C; }
constexpr int m_ev(int b, int stage) {
  const int t = b - m_g(1) - stage;
  return t >= 0 && t % M_C == 0 && t / M_C < 7 ? t / M_C : -1;
}
constexpr int m_ops(int b) {
  return (b + M_LA < 56 ? 2 : 0) + (m_ev(b, 0) >= 0 ? 2 : 0) + (m_ev(b, 2) >= 0 ? 2 : 0);
}
constexpr int m_pos(int b) { int n = 2 * M_LA; for (int t = 0; t < b; ++t) n += m_ops(t); return n; }
constexpr int m_rd(int b) { return b < M_LA ? 2 * (b + 1) : m_pos(b - M_LA) + 2; }
template <int OFF> DEV void wr128(unsigned a, f32x4 v) { asm volatile("ds_write_b128 %0, %1 offset:%2" ::"v"(a), "a"(v), "n"(OFF) : "memory"); }
template <int OFF> DEV f32x4 rdf128(unsigned a) {
  f32x4 r; asm volatile("ds_read_b128 %0, %1 offset:%2" : "=v"(r) : "v"(a), "n"(OFF)); return r;
}

__global__ __launch_bounds__(256, 1) void k_fused(const u16* __restrict__ oF, const u16* __restrict__ wo, const u16* __restrict__ x,
                                                  u16* hF, float* stats, const u16* __restrict__ w1, const u16* __restrict__ b1,
                                                  u16* hm, const u16* __restrict__ w2, const u16* __restrict__ b2,
                                                  u16* __restrict__ out, unsigned* flags) {
  extern __shared__ __attribute__((aligned(16))) char smem[];
  TS(1, 0);
  const int tid = threadIdx.x, lane = tid & 63, wid = __builtin_amdgcn_readfirstlane(tid >> 6);
  const int r = lane & 15, g = lane >> 4, q = (lane & 15) >> 2, p = lane & 3;
  const int b = blockIdx.x, xcd = b & 7, j = b >> 3;
  const int m0 = (xcd * 4 + (j >> 3)) * 64, n0 = (j & 7) * 64;
  const int m1 = (xcd * 2 + (j >> 4)) * 128, n1 = (j & 15) * 112;
  unsigned* fl = flags + (xcd * 2 + (j >> 4)) * 64;
  const int kh = wid & 1, mh = wid >> 1;
  const unsigned sbase = (unsigned)(size_t)smem;
  const int l3 = lane >> 3, l7 = lane & 7;
  const int row = m0 + wid * 16 + r, colo = n0 + (g & 1) * 16 + (g >> 1) * 8;
  const unsigned wswE = l3 * 1024 + (((l7 >> 1) ^ ((l3 >> 1) & 1)) << 5) + (l7 & 1) * 16;
  const unsigned wswO = (l3 + 8) * 1024 + (((l7 >> 1) ^ (((l3 >> 1) & 1) | 2)) << 5) + (l7 & 1) * 16;
  unsigned lB[4];
  {
    const int f = (q >> 1) | ((g & 1) << 1);
#pragma unroll
    for (int n = 0; n < 4; ++n) lB[n] = sbase + (8 * g + q) * 128 + ((n ^ f) << 5) + p * 8;
  }
  bf16x4 wq[2][4][2];
  u32x4 hres[2];

  unsigned woff[8];
#pragma unroll
  for (int t = 0; t < 8; ++t) {
    const int kr = t * 4 + (lane >> 4), f = (kr & 3) | ((kr >> 1) & 4);
    woff[t] = kr * 3584 + (((lane & 15) ^ (f << 1)) << 4);
  }
  const char* wsrc = (const char*)w1 + (size_t)(kh * 256 + mh * 32) * 3584 + ((n1 * 2) & ~63);
  auto issueW1 = [&](auto xx) {
    constexpr int t = xx >> 3, e = xx & 7;
    char* dst = smem + (t < 3 ? kh * 49152 + t * 16384 : 98304 + kh * 16384) + mh * 8192;
    glds16(wsrc + (size_t)t * (64 * 3584) + woff[e], dst + e * 1024);
  };
  {
    const char* abase = (const char*)oF + ((size_t)(m0 >> 4) * 16 + wid * 4) * 1024;
    const char* wb0 = (const char*)wo + (size_t)(wid * 128) * 1024 + n0 * 2;
    char* wr = smem + 98304 + wid * 16384;
    bf16x8 xa[4][4];
    static_for<0, 4>([&](auto ss) {
      constexpr int s = ss;
      static_for<0, 4>([&](auto ii) { constexpr int i = ii; xa[s][i] = gld128<(s ^ (SKW * i)) * 1024>(lane * 16 + i * 16384, abase); });
      glds16(wb0 + s * 32768 + wswE, wr + s * 4096);
      glds16(wb0 + s * 32768 + wswO, wr + s * 4096 + 1024);
      glds16(wb0 + s * 32768 + 16384 + wswE, wr + s * 4096 + 2048);
      glds16(wb0 + s * 32768 + 16384 + wswO, wr + s * 4096 + 3072);
    });
    u32x4 rr[2];
    rr[0] = gldu4(x + (size_t)row * 512 + colo); rr[1] = gldu4(x + (size_t)row * 512 + colo + 32);
    constexpr int T = 34;
    f32x4 acc[4][4];
#pragma unroll
    for (int i = 0; i < 4; ++i)
#pragma unroll
      for (int n = 0; n < 4; ++n) acc[i][n] = f32x4{0, 0, 0, 0};
    const unsigned wo_ = 98304 + wid * 16384;
    auto rdB = [&](auto ss, auto nn) {
      constexpr int s = ss, n = nn;
      wq[s & 1][n][0] = rdtr<s * 4096>(lB[n] + wo_); wq[s & 1][n][1] = rdtr<s * 4096 + 512>(lB[n] + wo_);
    };
    auto tieA = [&](auto ss) { auto& X = xa[ss]; asm volatile("" : "+v"(X[0]), "+v"(X[1]), "+v"(X[2]), "+v"(X[3])); };
    wait_vm<T - 8>(); tieA(IC<0>{});
    TS(1, 1);
    static_for<0, 4>([&](auto nn) { rdB(IC<0>{}, nn); });
    static_for<0, 4>([&](auto ss) {
      constexpr int s = ss;
      if constexpr (s < 3) { wait_vm<T + 8 * s - 8 * (s + 2)>(); tieA(IC<s + 1>{}); }
      static_for<0, 4>([&](auto nn) {
        constexpr int n = nn;
        asm volatile("s_waitcnt lgkmcnt(%0)" ::"n"(s < 3 ? 6 : 6 - 2 * n) : "memory");
        { auto& X = wq[s & 1][n]; asm volatile("" : "+v"(X[0]), "+v"(X[1])); }
        const bf16x8 wf = cat(wq[s & 1][n][0], wq[s & 1][n][1]);
#pragma unroll
        for (int i = 0; i < 4; ++i) acc[i][n] = mfma(wf, xa[s][i], acc[i][n]);
        if constexpr (s < 3) { rdB(IC<s + 1>{}, nn); issueW1(IC<s * 8 + n * 2>{}); issueW1(IC<s * 8 + n * 2 + 1>{}); }
        SB();
      });
    });
    TS(1, 2);
    wait_vm<24>(); asm volatile("" : "+v"(rr[0]), "+v"(rr[1]));
#pragma unroll
    for (int i = 0; i < 4; ++i) {
      if (i != wid) {
#pragma unroll
        for (int n = 0; n < 4; ++n) *(f32x4*)(wr + (i * 4 + n) * 1024 + lane * 16) = acc[i][n];
      }
    }
    f32x4 a[4];
#pragma unroll
    for (int n = 0; n < 4; ++n) a[n] = wid == 0 ? acc[0][n] : wid == 1 ? acc[1][n] : wid == 2 ? acc[2][n] : acc[3][n];
    lbar();
#pragma unroll
    for (int v = 1; v < 4; ++v) {
      const char* src = smem + 98304 + ((wid + v) & 3) * 16384 + wid * 4096 + lane * 16;
#pragma unroll
      for (int n = 0; n < 4; ++n) a[n] += *(const f32x4*)(src + n * 1024);
    }
    TS(1, 3);
    float s = 0.f, sq = 0.f;
#pragma unroll
    for (int hh = 0; hh < 2; ++hh) {
      float v[8];
#pragma unroll
      for (int e = 0; e < 4; ++e) {
        auto pr = __builtin_amdgcn_permlane16_swap(__float_as_uint(a[2 * hh][e]), __float_as_uint(a[2 * hh + 1][e]), false, false);
        v[e] = __uint_as_float(pr[0]); v[4 + e] = __uint_as_float(pr[1]);
      }
#pragma unroll
      for (int e = 0; e < 4; ++e) hres[hh][e] = pk2(v[2 * e] + bflo(rr[hh][e]), v[2 * e + 1] + bfhi(rr[hh][e]));
      st128c((char*)hF + fragoff(row, colo + hh * 32, 16), hres[hh]);
#pragma unroll
      for (int e = 0; e < 4; ++e) { s = dot2(hres[hh][e], 0x3f803f80u, s); sq = dot2(hres[hh][e], hres[hh][e], sq); }
    }
    s = red_gv(s); sq = red_gv(sq);
    if (g == 0) st64c(stats + (((size_t)(row >> 4) * 8 + (j & 7)) * 16 + r) * 2, u32x2{__float_as_uint(s), __float_as_uint(sq)});
    wait_vm<0>();
    lbar();
    if (tid == 0) __hip_atomic_fetch_add(fl, 1u, __ATOMIC_RELAXED, __HIP_MEMORY_SCOPE_AGENT);
    TS(1, 5);
  }

  u32x4 bb[2];
  {
    static_for<24, 32>(issueW1);
    u32x2 bu[7];
#pragma unroll
    for (int n = 0; n < 7; ++n) bu[n] = gldu2(b1 + n1 + n * 16 + g * 4);
    bb[0] = gldu4(b2 + colo); bb[1] = gldu4(b2 + colo + 32);
    unsigned aB[7];
#pragma unroll
    for (int n = 0; n < 7; ++n) aB[n] = sbase + kh * 49152 + (8 * g + q) * 256 + (((n + (j & 1)) ^ (q | ((g & 1) << 2))) << 5) + p * 8;
    const unsigned dl = 49152 - kh * 32768;
    {
      const bf16x8 ones = {0x3f80, 0x3f80, 0x3f80, 0x3f80, 0x3f80, 0x3f80, 0x3f80, 0x3f80};
      bf16x4 wb[2][7][2];
      f32x4 cs[7];
#pragma unroll
      for (int n = 0; n < 7; ++n) cs[n] = f32x4{0, 0, 0, 0};
      static_for<0, 4>([&](auto tt) {
        constexpr int t = tt;
        if constexpr (t == 3) wait_vm<0>();
#pragma unroll
        for (int n = 0; n < 7; ++n) {
          const unsigned a = aB[n] + mh * 8192 + (t == 3 ? dl : 0);
          wb[t & 1][n][0] = rdtr<t * 16384>(a); wb[t & 1][n][1] = rdtr<t * 16384 + 1024>(a);
        }
        wait_lgkm0();
#pragma unroll
        for (int n = 0; n < 7; ++n) {
          auto& X = wb[t & 1][n]; asm volatile("" : "+v"(X[0]), "+v"(X[1]));
          cs[n] = mfma(cat(X[0], X[1]), ones, cs[n]);
        }
      });
      asm volatile("" : "+v"(bu[0]), "+v"(bu[1]), "+v"(bu[2]), "+v"(bu[3]), "+v"(bu[4]), "+v"(bu[5]), "+v"(bu[6]), "+v"(bb[0]), "+v"(bb[1]));
      if (r == 0) {
#pragma unroll
        for (int n = 0; n < 7; ++n) *(f32x4*)(smem + 131072 + wid * 448 + n * 64 + g * 16) = cs[n];
      }
    }
    if (wid == 0) poll_ge(fl, 16u);
    lbar();
    TS(2, 0);
    const char* abase = (const char*)hF + ((size_t)((m1 >> 4) + mh * 4) * 16 + kh * (8 - 2 * SKW)) * 1024;
    unsigned voff[2][4];
#pragma unroll
    for (int i = 0; i < 4; ++i) {
      voff[0][i] = lane * 16 + ((i + 2 * kh) & 3) * 16384 + SKW * kh * 4096;
      voff[1][i] = lane * 16 + ((i + 2 * kh) & 3) * 16384;
    }
    const int rb0 = (m1 >> 4) + mh * 4 + kh * 2;
    f32x2 sp[2][2];
#pragma unroll
    for (int i = 0; i < 2; ++i)
#pragma unroll
      for (int e = 0; e < 2; ++e) sp[i][e] = gldf2c(stats + (((size_t)(rb0 + i) * 8 + g * 2 + e) * 16 + r) * 2);
    bf16x8 xa[8][4];
    static_for<0, 8>([&](auto ss) {
      constexpr int s = ss;
      static_for<0, 4>([&](auto ii) {
        constexpr int i = ii;
        constexpr int u = (s & 3) ^ (SKW * i);
        if constexpr (s < 4) xa[s][i] = gld128c<u * 1024>(voff[u >> 1][i], abase + (s >> 2) * 4096);
        else xa[s][i] = gld128ca<u * 1024>(voff[u >> 1][i], abase + (s >> 2) * 4096);
      });
    });
    auto pfW2 = [&]() {
      const char* q2 = (const char*)w2 + (size_t)j * 57344 + lane * 64;
#pragma unroll
      for (int i = 0; i < 4; ++i) {
        const int u = wid * 4 + i;
        __builtin_amdgcn_global_load_lds((const unsigned*)(q2 + (u < 14 ? u : 13) * 4096), (unsigned*)(smem + 139264 + wid * 256), 4, 0, 0);
      }
    };
    if constexpr (X_PFAT < 0) pfW2();
    f32x4 c4[7];
#pragma unroll
    for (int n = 0; n < 7; ++n) {
      const char* cp = smem + 131072 + n * 64 + g * 16;
      c4[n] = (*(const f32x4*)cp + *(const f32x4*)(cp + 448)) + (*(const f32x4*)(cp + 896) + *(const f32x4*)(cp + 1344));
    }
    asm volatile("" : "+v"(c4[0]), "+v"(c4[1]), "+v"(c4[2]), "+v"(c4[3]), "+v"(c4[4]), "+v"(c4[5]), "+v"(c4[6]));
    f32x4 acc[4][7];
#pragma unroll
    for (int n = 0; n < 7; ++n) acc[0][n] = acc[1][n] = acc[2][n] = acc[3][n] = f32x4{0, 0, 0, 0};
    auto tieA = [&](auto ss) {
      auto& X = xa[ss];
      if constexpr (ss < 4) asm volatile("" : "+v"(X[0]), "+v"(X[1]), "+v"(X[2]), "+v"(X[3]));
      else asm volatile("" : "+a"(X[0]), "+a"(X[1]), "+a"(X[2]), "+a"(X[3]));
    };
    bf16x4 wr[8][2];
    f32x4 ex[2][2], wz[2][7];
    float mean[2], rs[2];
    unsigned pkd[2][2];
    const unsigned exw = sbase + 147456 + (wid ^ 1) * 4096 + lane * 16, exr = sbase + 147456 + wid * 4096 + lane * 16;
    auto rdB = [&](auto bb_) {
      constexpr int bk = bb_, s = m_s(bk), n = m_n(bk);
      const unsigned a = s < 6 ? aB[n] : aB[n] + dl;
      wr[bk & 7][0] = rdtr<s * 8192>(a); wr[bk & 7][1] = rdtr<s * 8192 + 1024>(a);
    };
    f32x2 z_[4], t_[4];
    char* hmw = (char*)hm + (size_t)(rb0 + (g & 1)) * (56 * 1024) + r * 16 + (n1 / 8 + (g >> 1)) * 256;
    auto piece = [&](auto nn, auto cc) {
      constexpr int n = nn, c = cc;
      if constexpr (c < 2) {
#pragma unroll
        for (int e = 0; e < 2; ++e)
          z_[2 * c + e] = (f32x2{acc[c][n][2 * e], acc[c][n][2 * e + 1]} + f32x2{ex[n & 1][c][2 * e], ex[n & 1][c][2 * e + 1]}) * rs[c] +
                          f32x2{wz[c][n][2 * e], wz[c][n][2 * e + 1]};
      } else if constexpr (c < 4) {
#pragma unroll
        for (int u = 2 * (c - 2); u < 2 * (c - 2) + 2; ++u) t_[u] = z_[u] * ((z_[u] * z_[u]) * -0.10294324f + -2.3022082f);
      } else if constexpr (c < 8) {
        t_[c - 4] = f32x2{__builtin_amdgcn_exp2f(t_[c - 4][0]), __builtin_amdgcn_exp2f(t_[c - 4][1])};
      } else if constexpr (c == 8) {
#pragma unroll
        for (int u = 0; u < 4; ++u) t_[u] = t_[u] + 1.f;
        t_[0][0] = __builtin_amdgcn_rcpf(t_[0][0]);
      } else if constexpr (c < 13) {
        constexpr int k = 2 * (c - 9) + 1;
        t_[k >> 1][k & 1] = __builtin_amdgcn_rcpf(t_[k >> 1][k & 1]);
        if constexpr (k + 1 < 8) t_[(k + 1) >> 1][(k + 1) & 1] = __builtin_amdgcn_rcpf(t_[(k + 1) >> 1][(k + 1) & 1]);
        else { z_[0] = z_[0] * t_[0]; z_[1] = z_[1] * t_[1]; }
      } else if constexpr (c == 13) {
        z_[2] = z_[2] * t_[2]; z_[3] = z_[3] * t_[3];
        pkd[0][0] = pk2(z_[0][0], z_[0][1]); pkd[0][1] = pk2(z_[1][0], z_[1][1]);
      } else if constexpr (c == 14) {
        pkd[1][0] = pk2(z_[2][0], z_[2][1]); pkd[1][1] = pk2(z_[3][0], z_[3][1]);
        auto s0 = __builtin_amdgcn_permlane16_swap(pkd[0][0], pkd[1][0], false, false);
        auto s1 = __builtin_amdgcn_permlane16_swap(pkd[0][1], pkd[1][1], false, false);
        st128c(hmw + n * 512, u32x4{s0[0], s1[0], s0[1], s1[1]});
      }
    };
    SB();
    static_for<0, M_LA>(rdB);
    static_for<0, 64>([&](auto bb_) {
      constexpr int bk = bb_;
      constexpr int e0 = m_ev(bk, 0), e1 = m_ev(bk, 1), e2 = m_ev(bk, 2);
      constexpr int st = m_ev(bk, 4) >= 0 ? 4 : m_ev(bk, 5) >= 0 ? 5 : m_ev(bk, 6) >= 0 ? 6 : m_ev(bk, 7) >= 0 ? 7 : -1;
      constexpr int en = st >= 0 ? m_ev(bk, st) : -1;
      if constexpr (bk < 56) {
        constexpr int s = m_s(bk), n = m_n(bk);
        if constexpr (n == 0) {
          wait_vm<32 + (s > X_PFAT ? 4 : 0) - 4 * (s + 1)>(); tieA(IC<s>{});
          if constexpr (s == X_PFAT) pfW2();
          if constexpr (s == 0) TS(2, 1);
        }
        asm volatile("s_waitcnt lgkmcnt(%0)" ::"n"(m_pos(bk) - m_rd(bk)) : "memory");
        { auto& X = wr[bk & 7]; asm volatile("" : "+v"(X[0]), "+v"(X[1])); }
      } else if constexpr (bk == 56) {
        TS(2, 2);
        asm volatile("s_nop 15\n\ts_nop 15" ::: "memory");
      }
      if constexpr (st == 4) {
        asm volatile("s_waitcnt lgkmcnt(%0)" ::"n"(m_pos(bk) - m_pos(bk - 1)) : "memory");
        asm volatile("" : "+v"(ex[en & 1][0]), "+v"(ex[en & 1][1]));
      }
      static_for<0, 4>([&](auto ii) {
        constexpr int i = ii;
        if constexpr (bk < 56) {
          constexpr int s = m_s(bk), n = m_n(bk);
          acc[i][n] = mfma(cat(wr[bk & 7][0], wr[bk & 7][1]), xa[s][i], acc[i][n]);
        }
        if constexpr (st >= 0) { piece(IC<en>{}, IC<(st - 4) * 4 + i>{}); SB(); }
      });
      if constexpr (bk + M_LA < 56) rdB(IC<bk + M_LA>{});
      if constexpr (e0 >= 0) { wr128<(e0 & 1) * 2048>(exw, acc[2][e0]); wr128<(e0 & 1) * 2048 + 1024>(exw, acc[3][e0]); }
      if constexpr (e2 >= 0) { ex[e2 & 1][0] = rdf128<(e2 & 1) * 2048>(exr); ex[e2 & 1][1] = rdf128<(e2 & 1) * 2048 + 1024>(exr); }
      if constexpr (e1 >= 0) {
        asm volatile("s_waitcnt lgkmcnt(%0)\n\ts_barrier" ::"n"(m_pos(bk + 1) - m_pos(bk)) : "memory");
      }
      if constexpr (bk == 1) {
        asm volatile("" : "+v"(sp[0][0]), "+v"(sp[0][1]), "+v"(sp[1][0]), "+v"(sp[1][1]));
#pragma unroll
        for (int i = 0; i < 2; ++i) {
          float sm = sp[i][0][0] + sp[i][1][0], sq = sp[i][0][1] + sp[i][1][1];
          sm = red_gv(sm); sq = red_gv(sq);
          const float a = sm * (1.f / 512.f);
          const float var = fmaxf(sq * (1.f / 512.f) - a * a, 0.f);
          const float r1 = rsqrtf(var + 1e-5f), r2 = rsqrtf(var * r1 * r1 + 1e-5f);
          rs[i] = r1 * r2; mean[i] = a * rs[i];
        }
      }
      if constexpr (bk >= 2 && bk < 9) {
        constexpr int n = bk - 2;
        const f32x4 bf = {bflo(bu[n][0]), bfhi(bu[n][0]), bflo(bu[n][1]), bfhi(bu[n][1])};
        wz[0][n] = bf - mean[0] * c4[n]; wz[1][n] = bf - mean[1] * c4[n];
        asm volatile("" : "+v"(wz[0][n]), "+v"(wz[1][n]));
      }
      SB();
    });
    TS(2, 4);
  }

  {
    const char* wb0 = (const char*)w2 + (size_t)(wid * 448) * 1024 + n0 * 2;
    char* wr = smem + wid * 40960;
    auto issueW = [&](auto ss) {
      constexpr int s = ss, so = (s % F_DW) * 4096;
      glds16(wb0 + s * 32768 + wswE, wr + so);
      glds16(wb0 + s * 32768 + wswO, wr + so + 1024);
      glds16(wb0 + s * 32768 + 16384 + wswE, wr + so + 2048);
      glds16(wb0 + s * 32768 + 16384 + wswO, wr + so + 3072);
    };
    wait_lgkm0();
    static_for<0, F_PRE>(issueW);
    wait_vm<4 * F_PRE>();
    TS(3, 4);
    bar();
    if (tid == 0) __hip_atomic_fetch_add(fl + 16 * 64, 1u, __ATOMIC_RELAXED, __HIP_MEMORY_SCOPE_AGENT);
    TS(2, 5);
    static_for<F_PRE, F_DW>(issueW);
    if (wid == 0) poll_ge(fl + 16 * 64, 16u);
    wait_vm<0>();
    bar();
    TS(3, 0);
    const char* abase = (const char*)hm + ((size_t)(m0 >> 4) * 56 + wid * 14) * 1024;
    bf16x8 xa[F_DA][4];
    auto issueA = [&](auto ss) {
      constexpr int s = ss;
      static_for<0, 4>([&](auto ii) { constexpr int i = ii; xa[s % F_DA][i] = gld128c<(s & 3) * 1024>(lane * 16 + i * 57344, abase + (s >> 2) * 4096); });
    };
    static_for<0, F_DA>(issueA);
    f32x4 acc[4][4];
#pragma unroll
    for (int i = 0; i < 4; ++i)
#pragma unroll
      for (int n = 0; n < 4; ++n) acc[i][n] = f32x4{0, 0, 0, 0};
    const unsigned wo_ = wid * 40960;
    auto rdB = [&](auto ss, auto nn) {
      constexpr int s = ss, n = nn, so = (s % F_DW) * 4096;
      wq[s & 1][n][0] = rdtr<so>(lB[n] + wo_); wq[s & 1][n][1] = rdtr<so + 512>(lB[n] + wo_);
    };
    auto tieA = [&](auto ss) { auto& X = xa[ss % F_DA]; asm volatile("" : "+v"(X[0]), "+v"(X[1]), "+v"(X[2]), "+v"(X[3])); };
    static_for<0, 4>([&](auto nn) { rdB(IC<0>{}, nn); });
    static_for<0, 14>([&](auto ss) {
      constexpr int s = ss;
      wait_vm<p4_n(s) - imax(p4_a(s), s < 13 ? p4_w(s + 1) : 0)>(); tieA(ss);
      if constexpr (s == 0) TS(3, 1);
      static_for<0, 4>([&](auto nn) {
        constexpr int n = nn;
        asm volatile("s_waitcnt lgkmcnt(%0)" ::"n"(s < 13 ? 6 : 6 - 2 * n) : "memory");
        { auto& X = wq[s & 1][n]; asm volatile("" : "+v"(X[0]), "+v"(X[1])); }
        const bf16x8 wf = cat(wq[s & 1][n][0], wq[s & 1][n][1]);
#pragma unroll
        for (int i = 0; i < 4; ++i) acc[i][n] = mfma(wf, xa[s % F_DA][i], acc[i][n]);
        if constexpr (s < 13) rdB(IC<s + 1>{}, nn);
        SB();
      });
      if constexpr (s + F_DA < 14) issueA(IC<s + F_DA>{});
      if constexpr (s + F_DW < 14) issueW(IC<s + F_DW>{});
      SB();
    });
    TS(3, 2);
#pragma unroll
    for (int i = 0; i < 4; ++i) {
      if (i != wid) {
#pragma unroll
        for (int n = 0; n < 4; ++n) *(f32x4*)(wr + (i * 4 + n) * 1024 + lane * 16) = acc[i][n];
      }
    }
    f32x4 a[4];
#pragma unroll
    for (int n = 0; n < 4; ++n) a[n] = wid == 0 ? acc[0][n] : wid == 1 ? acc[1][n] : wid == 2 ? acc[2][n] : acc[3][n];
    lbar();
#pragma unroll
    for (int v = 1; v < 4; ++v) {
      const char* src = smem + ((wid + v) & 3) * 40960 + wid * 4096 + lane * 16;
#pragma unroll
      for (int n = 0; n < 4; ++n) a[n] += *(const f32x4*)(src + n * 1024);
    }
    TS(3, 3);
#pragma unroll
    for (int hh = 0; hh < 2; ++hh) {
      float v[8];
#pragma unroll
      for (int e = 0; e < 4; ++e) {
        auto pr = __builtin_amdgcn_permlane16_swap(__float_as_uint(a[2 * hh][e]), __float_as_uint(a[2 * hh + 1][e]), false, false);
        v[e] = __uint_as_float(pr[0]); v[4 + e] = __uint_as_float(pr[1]);
      }
      u32x4 ov;
#pragma unroll
      for (int e = 0; e < 4; ++e)
        ov[e] = pk2(v[2 * e] + bflo(hres[hh][e]) + bflo(bb[hh][e]), v[2 * e + 1] + bfhi(hres[hh][e]) + bfhi(bb[hh][e]));
      *(u32x4*)(out + (size_t)row * 512 + colo + hh * 32) = ov;
    }
    TS(3, 5);
  }
}
extern "C" void kernel_launch(void* const* d_in, const int* in_sizes, int n_in, void* d_out, int out_size, void* d_ws, size_t ws_size,
                              hipStream_t stream) {
  const u16* x = (const u16*)d_in[0];
  const u16* wqkv = (const u16*)d_in[1];
  const u16* wo = (const u16*)d_in[2];
  const u16* w1 = (const u16*)d_in[4];
  const u16* b1 = (const u16*)d_in[5];
  const u16* w2 = (const u16*)d_in[6];
  const u16* b2 = (const u16*)d_in[7];
  u16* o = (u16*)d_ws;
  u16* h = o + 2048 * 512;
  u16* hm = h + 2048 * 512;
  float* stats = (float*)(hm + 2048 * 1792);
  unsigned* flags = (unsigned*)(stats + 2048 * 32);
  u16* out = (u16*)d_out;
  (void)hipFuncSetAttribute((const void*)k_qkv_attn, hipFuncAttributeMaxDynamicSharedMemorySize, P1_LDS);
  (void)hipFuncSetAttribute((const void*)k_fused, hipFuncAttributeMaxDynamicSharedMemorySize, F_LDS);
  k_qkv_attn<<<256, P1_THREADS, P1_LDS, stream>>>(x, wqkv, o, wo, w1, w2, flags);
  k_fused<<<256, 256, F_LDS, stream>>>(o, wo, x, h, stats, w1, b1, hm, w2, b2, out, flags);
}
```
